```python
import torch

def _softmax(x, axis=-1):
    return torch.softmax(x, dim=axis)
def _gelu(x):
    return torch.nn.functional.gelu(x, approximate="tanh")
def _silu(x):
    return torch.nn.functional.silu(x)
def _sigmoid(x):
    return torch.sigmoid(x)
def _layernorm(x, axis=-1, eps=1e-5):
    m = x.mean(dim=axis, keepdim=True)
    v = x.var(dim=axis, keepdim=True, unbiased=False)
    return (x - m) / torch.sqrt(v + eps)
def _scatter_add(tgt, idx, src, axis=0):
    return tgt.clone().index_add_(axis, idx, src)
def _segment_sum(src, seg, n):
    out = torch.zeros((n,) + src.shape[1:], dtype=src.dtype)
    return out.index_add_(0, seg, src)
def _pad(x, pw):
    flat = tuple(v for pair in reversed(pw) for v in pair)
    return torch.nn.functional.pad(x, flat)

kernel_name = 'standalone__gqa_softmax__near_ridge'

def setup_inputs():

    _w = (lambda a: (lambda t: t.to(torch.bfloat16) if t.is_floating_point() else t)(torch.as_tensor(a)))
    B, Hq, Hkv, Sq, Sk, D = 8, 32, 8, 80, 1024, 128
    return {
        "q": _w((lambda *s: torch.randn(*s, dtype=torch.bfloat16) * 0.8)(B, Hq, Sq, D)),
        "k": _w((lambda *s: torch.randn(*s, dtype=torch.bfloat16) * 0.8)(B, Hkv, Sk, D)),
        "v": _w((lambda *s: torch.randn(*s, dtype=torch.bfloat16) * 0.05)(B, Hkv, Sk, D)),
    }

def reference(q, k, v):
    B, Hq, Sq, D = q.shape
    Hkv = k.shape[1]
    rep = Hq // Hkv
    k = (lambda x, n, axis: torch.repeat_interleave(x, n, dim=axis))(k, rep, axis=1)
    v = (lambda x, n, axis: torch.repeat_interleave(x, n, dim=axis))(v, rep, axis=1)
    scale = D ** -0.5
    scores = torch.einsum("bhqd,bhkd->bhqk", q, k) * scale
    p = _softmax(scores, axis=-1)
    return torch.einsum("bhqk,bhkd->bhqd", p, v)
```

```cpp
#include <hip/hip_runtime.h>
#include <hip/hip_bf16.h>
#include <stdint.h>

typedef __bf16   bf16x8 __attribute__((ext_vector_type(8)));
typedef __bf16   bf16x2 __attribute__((ext_vector_type(2)));
typedef short    s16x4  __attribute__((ext_vector_type(4)));
typedef float    f32x4  __attribute__((ext_vector_type(4)));
typedef float    f32x2  __attribute__((ext_vector_type(2)));
typedef unsigned u32x4  __attribute__((ext_vector_type(4)));
typedef unsigned u32x2  __attribute__((ext_vector_type(2)));

#define DI __device__ __forceinline__
#define SBAR() __builtin_amdgcn_sched_barrier(0)

namespace gqa {
constexpr int NB = 8, HQ = 32, HKV = 8, SQ = 80, SK = 1024, DH = 128;
constexpr int NQT = 5, NDT = 8;
constexpr int KPW = 256, TK = 32, NT = KPW / TK;
constexpr float CSC = 0.08838834764831845f * 1.4426950408889634f;
constexpr float INV_CSC = 1.0f / CSC;
constexpr float THR = 8.0f;
constexpr unsigned WAVE_LDS = 32768, SLOT = 8192, VOFF = 16384;
constexpr unsigned ML_OFF = 131072;
constexpr unsigned AL_OFF = ML_OFF + 4 * SQ * 8;
constexpr unsigned LDS_BYTES = AL_OFF + 4 * NQT * 64 * 4;
constexpr unsigned XREG = 3 * 10240;

DI unsigned cvtpk(float lo, float hi) {
  f32x2 v = {lo, hi}; bf16x2 b = __builtin_convertvector(v, bf16x2); return __builtin_bit_cast(unsigned, b);
}
DI float max3f(float a, float b, float c) { float r; asm("v_max3_f32 %0, %1, %2, %3" : "=v"(r) : "v"(a), "v"(b), "v"(c)); return r; }
DI float max2f(float a, float b) { float r; asm("v_max_f32_e32 %0, %1, %2" : "=v"(r) : "v"(a), "v"(b)); return r; }
DI float xl_max(float x) {
  auto r = __builtin_amdgcn_permlane32_swap(__float_as_uint(x), __float_as_uint(x), false, false);
  x = max2f(__uint_as_float(r[0]), __uint_as_float(r[1]));
  auto q = __builtin_amdgcn_permlane16_swap(__float_as_uint(x), __float_as_uint(x), false, false);
  return max2f(__uint_as_float(q[0]), __uint_as_float(q[1]));
}
DI float xl_sum(float x) {
  auto r = __builtin_amdgcn_permlane32_swap(__float_as_uint(x), __float_as_uint(x), false, false);
  x = __uint_as_float(r[0]) + __uint_as_float(r[1]);
  auto q = __builtin_amdgcn_permlane16_swap(__float_as_uint(x), __float_as_uint(x), false, false);
  return __uint_as_float(q[0]) + __uint_as_float(q[1]);
}
DI void glds4(unsigned m0v, unsigned v0, unsigned v1, unsigned v2, unsigned v3, __amdgpu_buffer_rsrc_t srd, unsigned soff) {
  asm volatile("s_mov_b32 m0, %0\n\ts_nop 0\n\t"
               "buffer_load_dwordx4 %1, %5, %6 offen lds\n\t"
               "buffer_load_dwordx4 %2, %5, %6 offen offset:1024 lds\n\t"
               "buffer_load_dwordx4 %3, %5, %6 offen offset:2048 lds\n\t"
               "buffer_load_dwordx4 %4, %5, %6 offen offset:3072 lds"
               :: "s"(m0v), "v"(v0), "v"(v1), "v"(v2), "v"(v3), "s"(srd), "s"(soff) : "m0", "memory");
}
template <int N> DI void wait_vm() { asm volatile("s_waitcnt vmcnt(%0)" :: "i"(N) : "memory"); }
DI void wait_lgkm0() { asm volatile("s_waitcnt lgkmcnt(0)" ::: "memory"); }
DI void mfma_qk(f32x4& d, const bf16x8& a, const bf16x8& b) { asm volatile("v_mfma_f32_16x16x32_bf16 %0, %1, %2, %0" : "+v"(d) : "v"(a), "a"(b)); }
DI void mfma_qk0(f32x4& d, const bf16x8& a, const bf16x8& b) { asm volatile("v_mfma_f32_16x16x32_bf16 %0, %1, %2, 0" : "=&v"(d) : "v"(a), "a"(b)); }
DI void mfma_pv(f32x4& o, const bf16x8& a, const bf16x8& b) { asm volatile("v_mfma_f32_16x16x32_bf16 %0, %1, %2, %0" : "+a"(o) : "v"(a), "v"(b)); }
DI void nop8() { asm volatile("s_nop 7"); }

struct St {
  bf16x8 qf[NQT][4];
  f32x4 o[NDT][NQT];
  f32x4 s[2][2][NQT];
  bf16x8 kf[2][4];
  bf16x8 vf[NDT];
  u32x4 p01[2][2];
  u32x4 p234[3];
  float mc[NQT], lim[NQT], l[NQT];
  float* alds;
  int resc;
  unsigned kb0, vb0;
  unsigned ksrc[4], vsrc[2];
  unsigned ldsw;
  __amdgpu_buffer_rsrc_t srdK, srdV;
};

template <bool ISV> DI void issue_tile(St& c, int t) {
  const unsigned m0b = c.ldsw + (ISV ? VOFF : 0u) + (unsigned)(t & 1) * SLOT;
  const unsigned sof = (unsigned)t * (TK * 256u);
#pragma unroll
  for (int h = 0; h < 2; ++h) {
    if (ISV) glds4(m0b + h * 4096u, c.vsrc[0], c.vsrc[1], c.vsrc[0], c.vsrc[1], c.srdV, sof + h * 4096u);
    else glds4(m0b + h * 4096u, c.ksrc[0], c.ksrc[1], c.ksrc[2], c.ksrc[3], c.srdK, sof + h * 4096u);
  }
}
template <int SL> DI void read_kf(St& c, const char* smem) {
  unsigned kb = c.kb0; asm volatile("" : "+v"(kb));
#pragma unroll
  for (int kt = 0; kt < 2; ++kt)
#pragma unroll
    for (int s = 0; s < 4; ++s)
      c.kf[kt][s] = *reinterpret_cast<const bf16x8*>(smem + (kb ^ (unsigned)(s << 6)) + SL * SLOT + kt * 4096);
}
DI s16x4 trread(const char* p) {
#if defined(__HIP_DEVICE_COMPILE__)
  return __builtin_amdgcn_ds_read_tr16_b64_v4i16((__attribute__((address_space(3))) s16x4*)(p));
#else
  return s16x4{};
#endif
}
template <int SL> DI void read_vf(St& c, const char* smem) {
  unsigned vb = c.vb0; asm volatile("" : "+v"(vb));
#pragma unroll
  for (int dt = 0; dt < NDT; ++dt) {
    const unsigned va = vb ^ (unsigned)(dt << 5);
    s16x4 lo = trread(smem + va + VOFF + SL * SLOT);
    s16x4 hi = trread(smem + va + VOFF + SL * SLOT + 4096);
    typedef short s16x8 __attribute__((ext_vector_type(8)));
    s16x8 w = __builtin_shufflevector(lo, hi, 0, 1, 2, 3, 4, 5, 6, 7);
    c.vf[dt] = __builtin_bit_cast(bf16x8, w);
  }
}
#define PIN(x) asm volatile("" : "+v"(x))
template <int PB> DI void sm_fma(St& c, int qt, int e) { c.s[PB][e >> 2][qt][e & 3] = fmaf(c.s[PB][e >> 2][qt][e & 3], CSC, -c.mc[qt]); }
template <int PB> DI void sm_exp(St& c, int qt, int e) { c.s[PB][e >> 2][qt][e & 3] = __builtin_amdgcn_exp2f(c.s[PB][e >> 2][qt][e & 3]); }
template <int PB> DI void sm_add(St& c, int qt, int e) { float x = c.l[qt] + c.s[PB][e >> 2][qt][e & 3]; PIN(x); c.l[qt] = x; }
template <int PB> DI unsigned sm_pack(St& c, int qt, int j) { unsigned x = cvtpk(c.s[PB][j >> 1][qt][(j & 1) * 2], c.s[PB][j >> 1][qt][(j & 1) * 2 + 1]); PIN(x); return x; }

template <int PB> DI void sm2_step(St& c, int k) {
  if (k < 24) sm_fma<PB>(c, 2 + k / 8, k % 8);
  if (k >= 1 && k - 1 < 24) sm_exp<PB>(c, 2 + (k - 1) / 8, (k - 1) % 8);
  if (k >= 2 && k - 2 < 24) { const int e = k - 2; sm_add<PB>(c, 2 + e / 8, e % 8); if (e & 1) c.p234[e / 8][(e % 8) >> 1] = sm_pack<PB>(c, 2 + e / 8, (e % 8) >> 1); }
}
template <int PB> DI void sm1_step(St& c, int k) {
  if (k < 16) sm_fma<PB>(c, k / 8, k % 8);
  if (k >= 1 && k - 1 < 16) sm_exp<PB>(c, (k - 1) / 8, (k - 1) % 8);
  if (k >= 2 && k - 2 < 16) { const int e = k - 2; sm_add<PB>(c, e / 8, e % 8); if (e & 1) c.p01[PB][e / 8][(e % 8) >> 1] = sm_pack<PB>(c, e / 8, (e % 8) >> 1); }
}
template <int PB> DI float tile_max(St& c, int qt) {
  float m = max3f(c.s[PB][0][qt][0], c.s[PB][0][qt][1], c.s[PB][0][qt][2]);
  m = max3f(m, c.s[PB][0][qt][3], c.s[PB][1][qt][0]);
  m = max3f(m, c.s[PB][1][qt][1], c.s[PB][1][qt][2]);
  return max2f(m, c.s[PB][1][qt][3]);
}
DI void take_max(St& c, const float (&mx)[NQT]) {
#pragma unroll
  for (int qt = 0; qt < NQT; ++qt) {
    const float mnew = fmaxf(c.mc[qt], xl_max(mx[qt]) * CSC);
    const float a = __builtin_amdgcn_exp2f(c.mc[qt] - mnew);
    c.mc[qt] = mnew; c.lim[qt] = (mnew + THR) * INV_CSC; c.l[qt] *= a; c.alds[qt * 64] = a;
  }
}

template <int PA, bool FIRST> DI void phaseA(St& c, const char* smem, int t) {
  wait_lgkm0();
  issue_tile<false>(c, t + 2);
  if (!FIRST) { wait_vm<24>(); read_vf<PA ^ 1>(c, smem); }
  SBAR();
#pragma unroll
  for (int i = 0; i < 40; ++i) {
    const int ss = i / 10, kt = (i % 10) / 5, qt = i % 5;
    if (ss == 0) mfma_qk0(c.s[PA][kt][qt], c.kf[kt][ss], c.qf[qt][ss]);
    else mfma_qk(c.s[PA][kt][qt], c.kf[kt][ss], c.qf[qt][ss]);
    if (!FIRST) { if (i < 27) sm2_step<PA ^ 1>(c, i); }
    SBAR();
  }
}
template <int PB, bool FIRST> DI void phaseB(St& c, const char* smem, int t) {
  wait_lgkm0();
  issue_tile<true>(c, t + 1);
  wait_vm<24>();
  read_kf<PB ^ 1>(c, smem);
  SBAR();
  float mx[NQT];
  bool need = false;
#pragma unroll
  for (int i = 0; i < 40; ++i) {
    const int qt = i / 8, dt = i % 8;
    if (!FIRST) {
      const bf16x8 pf = __builtin_bit_cast(bf16x8, qt < 2 ? c.p01[PB ^ 1][qt] : c.p234[qt - 2]);
      mfma_pv(c.o[dt][qt], c.vf[dt], pf);
    }
    if (i >= 2 && i < 7) { mx[i - 2] = tile_max<PB>(c, i - 2); need = need || (mx[i - 2] > c.lim[i - 2]); }
    if (i == 7) {
      c.resc = 0;
      if (__builtin_expect(__any(need), 0)) { take_max(c, mx); c.resc = 1; }
    }
    if (i >= 8 && i < 8 + 19) sm1_step<PB>(c, i - 8);
    SBAR();
  }
  if (!FIRST) {
    if (__builtin_expect(c.resc, 0)) {
      nop8();
#pragma unroll
      for (int qt = 0; qt < NQT; ++qt) { const float a = c.alds[qt * 64];
#pragma unroll
        for (int dt = 0; dt < NDT; ++dt) { f32x4 t = c.o[dt][qt]; asm volatile("" : "+v"(t)); t *= a; asm volatile("" : "+a"(t)); c.o[dt][qt] = t; } }
    }
  }
}

__global__ __launch_bounds__(256, 1) void attn_fwd(const uint16_t* __restrict__ Qg, const uint16_t* __restrict__ Kg,
                                                   const uint16_t* __restrict__ Vg, uint16_t* __restrict__ Og) {
  __shared__ __attribute__((aligned(16))) char smem[LDS_BYTES];
  const int tid = threadIdx.x, lane = tid & 63;
  const int wave = __builtin_amdgcn_readfirstlane(tid >> 6);
  const int bid = blockIdx.x, xcd = bid & 7, jj = bid >> 3;
  const int pair = xcd + 8 * (jj >> 2), hg = jj & 3;
  const int bq = (pair >> 3) * HQ + (pair & 7) * 4 + hg;
  const int g = lane >> 4, l15 = lane & 15;
  St c;
  c.ldsw = (unsigned)(uintptr_t)smem + (unsigned)wave * WAVE_LDS;
  const unsigned wofs = (unsigned)wave * WAVE_LDS;
  c.alds = reinterpret_cast<float*>(smem + AL_OFF) + wave * (NQT * 64) + lane;
  c.kb0 = wofs + l15 * 256 + ((g ^ l15) << 4);
  {
    const int q = l15 >> 2, p = l15 & 3, row = 4 * g + q, x = (row & 7) << 1;
    c.vb0 = wofs + row * 256 + (x << 4) + (p >> 1) * 16 + (p & 1) * 8;
  }
#pragma unroll
  for (int i = 0; i < 4; ++i) { const int r = 4 * i + g; c.ksrc[i] = g * 256 + ((l15 ^ (r & 15)) << 4); }
#pragma unroll
  for (int i = 0; i < 2; ++i) { const int r = 4 * i + g; c.vsrc[i] = g * 256 + ((l15 ^ ((r & 7) << 1)) << 4); }
  const size_t kvoff = ((size_t)pair * SK + (size_t)wave * KPW) * DH;
  c.srdK = __builtin_amdgcn_make_buffer_rsrc((void*)(Kg + kvoff), (short)0, KPW * 256, 0x00020000);
  c.srdV = __builtin_amdgcn_make_buffer_rsrc((void*)(Vg + kvoff), (short)0, KPW * 256, 0x00020000);
  issue_tile<false>(c, 0);
  const uint16_t* Qh = Qg + (size_t)bq * SQ * DH;
#pragma unroll
  for (int qt = 0; qt < NQT; ++qt)
#pragma unroll
    for (int s = 0; s < 4; ++s) c.qf[qt][s] = *reinterpret_cast<const bf16x8*>(Qh + (qt * 16 + l15) * DH + 32 * s + 8 * g);
  issue_tile<false>(c, 1);
  issue_tile<true>(c, 0);
#pragma unroll
  for (int qt = 0; qt < NQT; ++qt) { c.mc[qt] = -__builtin_inff(); c.lim[qt] = -__builtin_inff(); c.l[qt] = 0.f; }
#pragma unroll
  for (int dt = 0; dt < NDT; ++dt)
#pragma unroll
    for (int qt = 0; qt < NQT; ++qt) c.o[dt][qt] = f32x4{0.f, 0.f, 0.f, 0.f};
  wait_vm<16>();
  read_kf<0>(c, smem);
  phaseA<0, true>(c, smem, 0);
  phaseB<0, true>(c, smem, 0);
  for (int t = 1; t < NT - 1; t += 2) {
    phaseA<1, false>(c, smem, t);
    phaseB<1, false>(c, smem, t);
    phaseA<0, false>(c, smem, t + 1);
    phaseB<0, false>(c, smem, t + 1);
  }
  phaseA<1, false>(c, smem, NT - 1);
  phaseB<1, false>(c, smem, NT - 1);
  wait_vm<8>();
  read_vf<1>(c, smem);
#pragma unroll
  for (int k = 0; k < 27; ++k) sm2_step<1>(c, k);
  nop8();
#pragma unroll
  for (int qt = 0; qt < NQT; ++qt)
#pragma unroll
    for (int dt = 0; dt < NDT; ++dt) {
      const bf16x8 pf = __builtin_bit_cast(bf16x8, qt < 2 ? c.p01[1][qt] : c.p234[qt - 2]);
      mfma_pv(c.o[dt][qt], c.vf[dt], pf);
    }
  nop8();
  wait_vm<0>();
#pragma unroll
  for (int qt = 0; qt < NQT; ++qt) c.l[qt] = xl_sum(c.l[qt]);
  if (lane < 16) {
#pragma unroll
    for (int qt = 0; qt < NQT; ++qt) *reinterpret_cast<f32x2*>(smem + ML_OFF + (wave * SQ + qt * 16 + lane) * 8) = f32x2{c.mc[qt], c.l[qt]};
  }
  __syncthreads();
  float fsc[NQT];
#pragma unroll
  for (int qt = 0; qt < NQT; ++qt) {
    f32x2 v[4];
#pragma unroll
    for (int w = 0; w < 4; ++w) v[w] = *reinterpret_cast<const f32x2*>(smem + ML_OFF + (w * SQ + qt * 16 + l15) * 8);
    const float M = fmaxf(fmaxf(v[0][0], v[1][0]), fmaxf(v[2][0], v[3][0]));
    float L = 0.f;
#pragma unroll
    for (int w = 0; w < 4; ++w) L += v[w][1] * __builtin_amdgcn_exp2f(v[w][0] - M);
    const float f = __builtin_amdgcn_exp2f(c.mc[qt] - M) / L;
    fsc[qt] = f;
  }
#pragma unroll
  for (int tw = 0; tw < 4; ++tw) {
    if (tw != wave) {
      const int sl = wave < tw ? wave : wave - 1;
#pragma unroll
      for (int dl = 0; dl < 2; ++dl)
#pragma unroll
        for (int qt = 0; qt < NQT; ++qt)
        { f32x4 t = c.o[2 * tw + dl][qt]; asm volatile("" : "+v"(t)); t *= fsc[qt]; asm volatile("" : "+v"(t));
          *reinterpret_cast<f32x4*>(smem + tw * XREG + sl * 10240 + ((dl * 5 + qt) * 64 + lane) * 16) = t; }
    }
  }
  __syncthreads();
  int l15e = l15, ge = g; asm volatile("" : "+v"(l15e), "+v"(ge));
  uint16_t* Oh = Og + (size_t)bq * SQ * DH;
#pragma unroll
  for (int tw = 0; tw < 4; ++tw) {
    if (tw == wave) {
#pragma unroll
      for (int dl = 0; dl < 2; ++dl)
#pragma unroll
        for (int qt = 0; qt < NQT; ++qt) {
          f32x4 a = c.o[2 * tw + dl][qt]; asm volatile("" : "+v"(a)); a *= fsc[qt];
#pragma unroll
          for (int sl = 0; sl < 3; ++sl) a += *reinterpret_cast<const f32x4*>(smem + tw * XREG + sl * 10240 + ((dl * 5 + qt) * 64 + lane) * 16);
          u32x2 w = {cvtpk(a[0], a[1]), cvtpk(a[2], a[3])};
          *reinterpret_cast<u32x2*>(Oh + (qt * 16 + l15e) * DH + 32 * tw + 16 * dl + 4 * ge) = w;
        }
    }
  }
}
}

extern "C" void kernel_launch(void* const* d_in, const int* in_sizes, int n_in, void* d_out, int out_size, void* d_ws, size_t ws_size,
                              hipStream_t stream) {
  (void)in_sizes; (void)n_in; (void)out_size; (void)d_ws; (void)ws_size;
  gqa::attn_fwd<<<dim3(gqa::NB * gqa::HQ), dim3(256), 0, stream>>>((const uint16_t*)d_in[0], (const uint16_t*)d_in[1], (const uint16_t*)d_in[2], (uint16_t*)d_out);
}
```

```cpp
#include <hip/hip_runtime.h>
#include <hip/hip_bf16.h>
#include <stdint.h>

typedef __bf16   bf16x8 __attribute__((ext_vector_type(8)));
typedef __bf16   bf16x2 __attribute__((ext_vector_type(2)));
typedef short    s16x4  __attribute__((ext_vector_type(4)));
typedef float    f32x4  __attribute__((ext_vector_type(4)));
typedef float    f32x2  __attribute__((ext_vector_type(2)));
typedef unsigned u32x4  __attribute__((ext_vector_type(4)));
typedef unsigned u32x2  __attribute__((ext_vector_type(2)));

#define DI __device__ __forceinline__
#define SBAR() __builtin_amdgcn_sched_barrier(0)

namespace gqa {
constexpr int NB = 8, HQ = 32, HKV = 8, SQ = 80, SK = 1024, DH = 128;
constexpr int NQT = 5, NDT = 8;
constexpr int KPW = 256, TK = 32, NT = KPW / TK;
constexpr float CSC = 0.08838834764831845f * 1.4426950408889634f;
constexpr float INV_CSC = 1.0f / CSC;
constexpr float THR = 8.0f;
constexpr unsigned WAVE_LDS = 32768, SLOT = 8192, VOFF = 16384;
constexpr unsigned ML_OFF = 131072;
constexpr unsigned AL_OFF = ML_OFF + 4 * SQ * 8;
constexpr unsigned LDS_BYTES = AL_OFF + 4 * NQT * 64 * 4;
constexpr unsigned XREG = 3 * 10240;

DI unsigned cvtpk(float lo, float hi) {
  f32x2 v = {lo, hi}; bf16x2 b = __builtin_convertvector(v, bf16x2); return __builtin_bit_cast(unsigned, b);
}
DI float max3f(float a, float b, float c) { float r; asm("v_max3_f32 %0, %1, %2, %3" : "=v"(r) : "v"(a), "v"(b), "v"(c)); return r; }
DI float max2f(float a, float b) { float r; asm("v_max_f32_e32 %0, %1, %2" : "=v"(r) : "v"(a), "v"(b)); return r; }
DI float xl_max(float x) {
  auto r = __builtin_amdgcn_permlane32_swap(__float_as_uint(x), __float_as_uint(x), false, false);
  x = max2f(__uint_as_float(r[0]), __uint_as_float(r[1]));
  auto q = __builtin_amdgcn_permlane16_swap(__float_as_uint(x), __float_as_uint(x), false, false);
  return max2f(__uint_as_float(q[0]), __uint_as_float(q[1]));
}
DI float xl_sum(float x) {
  auto r = __builtin_amdgcn_permlane32_swap(__float_as_uint(x), __float_as_uint(x), false, false);
  x = __uint_as_float(r[0]) + __uint_as_float(r[1]);
  auto q = __builtin_amdgcn_permlane16_swap(__float_as_uint(x), __float_as_uint(x), false, false);
  return __uint_as_float(q[0]) + __uint_as_float(q[1]);
}
DI void glds4(unsigned m0v, unsigned v0, unsigned v1, unsigned v2, unsigned v3, __amdgpu_buffer_rsrc_t srd, unsigned soff) {
  asm volatile("s_mov_b32 m0, %0\n\ts_nop 0\n\t"
               "buffer_load_dwordx4 %1, %5, %6 offen nt lds\n\t"
               "buffer_load_dwordx4 %2, %5, %6 offen offset:1024 nt lds\n\t"
               "buffer_load_dwordx4 %3, %5, %6 offen offset:2048 nt lds\n\t"
               "buffer_load_dwordx4 %4, %5, %6 offen offset:3072 nt lds"
               :: "s"(m0v), "v"(v0), "v"(v1), "v"(v2), "v"(v3), "s"(srd), "s"(soff) : "m0", "memory");
}
template <int N> DI void wait_vm() { asm volatile("s_waitcnt vmcnt(%0)" :: "i"(N) : "memory"); }
DI void wait_lgkm0() { asm volatile("s_waitcnt lgkmcnt(0)" ::: "memory"); }
DI void mfma_qk(f32x4& d, const bf16x8& a, const bf16x8& b) { asm volatile("v_mfma_f32_16x16x32_bf16 %0, %1, %2, %0" : "+v"(d) : "v"(a), "a"(b)); }
DI void mfma_qk0(f32x4& d, const bf16x8& a, const bf16x8& b) { asm volatile("v_mfma_f32_16x16x32_bf16 %0, %1, %2, 0" : "=&v"(d) : "v"(a), "a"(b)); }
DI void mfma_pv(f32x4& o, const bf16x8& a, const bf16x8& b) { asm volatile("v_mfma_f32_16x16x32_bf16 %0, %1, %2, %0" : "+a"(o) : "v"(a), "v"(b)); }
DI void nop8() { asm volatile("s_nop 7"); }

struct St {
  bf16x8 qf[NQT][4];
  f32x4 o[NDT][NQT];
  f32x4 s[2][2][NQT];
  bf16x8 kf[2][4];
  bf16x8 vf[NDT];
  u32x4 p01[2][2];
  u32x4 p234[3];
  float mc[NQT], lim[NQT], l[NQT];
  float* alds;
  int resc;
  unsigned kb0, vb0;
  unsigned ksrc[4], vsrc[2];
  unsigned ldsw;
  __amdgpu_buffer_rsrc_t srdK, srdV;
};

template <bool ISV> DI void issue_tile(St& c, int t) {
  const unsigned m0b = c.ldsw + (ISV ? VOFF : 0u) + (unsigned)(t & 1) * SLOT;
  const unsigned sof = (unsigned)t * (TK * 256u);
#pragma unroll
  for (int h = 0; h < 2; ++h) {
    if (ISV) glds4(m0b + h * 4096u, c.vsrc[0], c.vsrc[1], c.vsrc[0], c.vsrc[1], c.srdV, sof + h * 4096u);
    else glds4(m0b + h * 4096u, c.ksrc[0], c.ksrc[1], c.ksrc[2], c.ksrc[3], c.srdK, sof + h * 4096u);
  }
}
template <int SL> DI void read_kf(St& c, const char* smem) {
  unsigned kb = c.kb0; asm volatile("" : "+v"(kb));
#pragma unroll
  for (int kt = 0; kt < 2; ++kt)
#pragma unroll
    for (int s = 0; s < 4; ++s)
      c.kf[kt][s] = *reinterpret_cast<const bf16x8*>(smem + (kb ^ (unsigned)(s << 6)) + SL * SLOT + kt * 4096);
}
DI s16x4 trread(const char* p) {
#if defined(__HIP_DEVICE_COMPILE__)
  return __builtin_amdgcn_ds_read_tr16_b64_v4i16((__attribute__((address_space(3))) s16x4*)(p));
#else
  return s16x4{};
#endif
}
template <int SL> DI void read_vf(St& c, const char* smem) {
  unsigned vb = c.vb0; asm volatile("" : "+v"(vb));
#pragma unroll
  for (int dt = 0; dt < NDT; ++dt) {
    const unsigned va = vb ^ (unsigned)(dt << 5);
    s16x4 lo = trread(smem + va + VOFF + SL * SLOT);
    s16x4 hi = trread(smem + va + VOFF + SL * SLOT + 4096);
    typedef short s16x8 __attribute__((ext_vector_type(8)));
    s16x8 w = __builtin_shufflevector(lo, hi, 0, 1, 2, 3, 4, 5, 6, 7);
    c.vf[dt] = __builtin_bit_cast(bf16x8, w);
  }
}
#define PIN(x) asm volatile("" : "+v"(x))
template <int PB> DI void sm_fma(St& c, int qt, int e) { c.s[PB][e >> 2][qt][e & 3] = fmaf(c.s[PB][e >> 2][qt][e & 3], CSC, -c.mc[qt]); }
template <int PB> DI void sm_exp(St& c, int qt, int e) { c.s[PB][e >> 2][qt][e & 3] = __builtin_amdgcn_exp2f(c.s[PB][e >> 2][qt][e & 3]); }
template <int PB> DI void sm_add(St& c, int qt, int e) { float x = c.l[qt] + c.s[PB][e >> 2][qt][e & 3]; PIN(x); c.l[qt] = x; }
template <int PB> DI unsigned sm_pack(St& c, int qt, int j) { unsigned x = cvtpk(c.s[PB][j >> 1][qt][(j & 1) * 2], c.s[PB][j >> 1][qt][(j & 1) * 2 + 1]); PIN(x); return x; }

template <int PB> DI void sm2_step(St& c, int k) {
  if (k < 24) sm_fma<PB>(c, 2 + k / 8, k % 8);
  if (k >= 1 && k - 1 < 24) sm_exp<PB>(c, 2 + (k - 1) / 8, (k - 1) % 8);
  if (k >= 2 && k - 2 < 24) { const int e = k - 2; sm_add<PB>(c, 2 + e / 8, e % 8); if (e & 1) c.p234[e / 8][(e % 8) >> 1] = sm_pack<PB>(c, 2 + e / 8, (e % 8) >> 1); }
}
template <int PB> DI void sm1_step(St& c, int k) {
  if (k < 16) sm_fma<PB>(c, k / 8, k % 8);
  if (k >= 1 && k - 1 < 16) sm_exp<PB>(c, (k - 1) / 8, (k - 1) % 8);
  if (k >= 2 && k - 2 < 16) { const int e = k - 2; sm_add<PB>(c, e / 8, e % 8); if (e & 1) c.p01[PB][e / 8][(e % 8) >> 1] = sm_pack<PB>(c, e / 8, (e % 8) >> 1); }
}
template <int PB> DI float tile_max(St& c, int qt) {
  float m = max3f(c.s[PB][0][qt][0], c.s[PB][0][qt][1], c.s[PB][0][qt][2]);
  m = max3f(m, c.s[PB][0][qt][3], c.s[PB][1][qt][0]);
  m = max3f(m, c.s[PB][1][qt][1], c.s[PB][1][qt][2]);
  return max2f(m, c.s[PB][1][qt][3]);
}
DI void take_max(St& c, const float (&mx)[NQT]) {
#pragma unroll
  for (int qt = 0; qt < NQT; ++qt) {
    const float mnew = fmaxf(c.mc[qt], xl_max(mx[qt]) * CSC);
    const float a = __builtin_amdgcn_exp2f(c.mc[qt] - mnew);
    c.mc[qt] = mnew; c.lim[qt] = (mnew + THR) * INV_CSC; c.l[qt] *= a; c.alds[qt * 64] = a;
  }
}

template <int PA, bool FIRST> DI void phaseA(St& c, const char* smem, int t) {
  wait_lgkm0();
  issue_tile<false>(c, t + 2);
  if (!FIRST) { wait_vm<24>(); read_vf<PA ^ 1>(c, smem); }
  SBAR();
#pragma unroll
  for (int i = 0; i < 40; ++i) {
    const int ss = i / 10, kt = (i % 10) / 5, qt = i % 5;
    if (ss == 0) mfma_qk0(c.s[PA][kt][qt], c.kf[kt][ss], c.qf[qt][ss]);
    else mfma_qk(c.s[PA][kt][qt], c.kf[kt][ss], c.qf[qt][ss]);
    if (!FIRST) { if (i < 27) sm2_step<PA ^ 1>(c, i); }
    SBAR();
  }
}
template <int PB, bool FIRST> DI void phaseB(St& c, const char* smem, int t) {
  wait_lgkm0();
  issue_tile<true>(c, t + 1);
  wait_vm<24>();
  read_kf<PB ^ 1>(c, smem);
  SBAR();
  float mx[NQT];
  bool need = false;
#pragma unroll
  for (int i = 0; i < 40; ++i) {
    const int qt = i / 8, dt = i % 8;
    if (!FIRST) {
      const bf16x8 pf = __builtin_bit_cast(bf16x8, qt < 2 ? c.p01[PB ^ 1][qt] : c.p234[qt - 2]);
      mfma_pv(c.o[dt][qt], c.vf[dt], pf);
    }
    if (i >= 2 && i < 7) { mx[i - 2] = tile_max<PB>(c, i - 2); need = need || (mx[i - 2] > c.lim[i - 2]); }
    if (i == 7) {
      c.resc = 0;
      if (__builtin_expect(__any(need), 0)) { take_max(c, mx); c.resc = 1; }
    }
    if (i >= 8 && i < 8 + 19) sm1_step<PB>(c, i - 8);
    SBAR();
  }
  if (!FIRST) {
    if (__builtin_expect(c.resc, 0)) {
      nop8();
#pragma unroll
      for (int qt = 0; qt < NQT; ++qt) { const float a = c.alds[qt * 64];
#pragma unroll
        for (int dt = 0; dt < NDT; ++dt) { f32x4 t = c.o[dt][qt]; asm volatile("" : "+v"(t)); t *= a; asm volatile("" : "+a"(t)); c.o[dt][qt] = t; } }
    }
  }
}

__global__ __launch_bounds__(256, 1) void attn_fwd(const uint16_t* __restrict__ Qg, const uint16_t* __restrict__ Kg,
                                                   const uint16_t* __restrict__ Vg, uint16_t* __restrict__ Og) {
  __shared__ __attribute__((aligned(16))) char smem[LDS_BYTES];
  const int tid = threadIdx.x, lane = tid & 63;
  const int wave = __builtin_amdgcn_readfirstlane(tid >> 6);
  const int bid = blockIdx.x, xcd = bid & 7, jj = bid >> 3;
  const int pair = xcd + 8 * (jj >> 2), hg = jj & 3;
  const int bq = (pair >> 3) * HQ + (pair & 7) * 4 + hg;
  const int g = lane >> 4, l15 = lane & 15;
  St c;
  c.ldsw = (unsigned)(uintptr_t)smem + (unsigned)wave * WAVE_LDS;
  const unsigned wofs = (unsigned)wave * WAVE_LDS;
  c.alds = reinterpret_cast<float*>(smem + AL_OFF) + wave * (NQT * 64) + lane;
  c.kb0 = wofs + l15 * 256 + ((g ^ l15) << 4);
  {
    const int q = l15 >> 2, p = l15 & 3, row = 4 * g + q, x = (row & 7) << 1;
    c.vb0 = wofs + row * 256 + (x << 4) + (p >> 1) * 16 + (p & 1) * 8;
  }
#pragma unroll
  for (int i = 0; i < 4; ++i) { const int r = 4 * i + g; c.ksrc[i] = g * 256 + ((l15 ^ (r & 15)) << 4); }
#pragma unroll
  for (int i = 0; i < 2; ++i) { const int r = 4 * i + g; c.vsrc[i] = g * 256 + ((l15 ^ ((r & 7) << 1)) << 4); }
  const size_t kvoff = ((size_t)pair * SK + (size_t)wave * KPW) * DH;
  c.srdK = __builtin_amdgcn_make_buffer_rsrc((void*)(Kg + kvoff), (short)0, KPW * 256, 0x00020000);
  c.srdV = __builtin_amdgcn_make_buffer_rsrc((void*)(Vg + kvoff), (short)0, KPW * 256, 0x00020000);
  issue_tile<false>(c, 0);
  const uint16_t* Qh = Qg + (size_t)bq * SQ * DH;
#pragma unroll
  for (int qt = 0; qt < NQT; ++qt)
#pragma unroll
    for (int s = 0; s < 4; ++s) c.qf[qt][s] = *reinterpret_cast<const bf16x8*>(Qh + (qt * 16 + l15) * DH + 32 * s + 8 * g);
  issue_tile<false>(c, 1);
  issue_tile<true>(c, 0);
#pragma unroll
  for (int qt = 0; qt < NQT; ++qt) { c.mc[qt] = -__builtin_inff(); c.lim[qt] = -__builtin_inff(); c.l[qt] = 0.f; }
#pragma unroll
  for (int dt = 0; dt < NDT; ++dt)
#pragma unroll
    for (int qt = 0; qt < NQT; ++qt) c.o[dt][qt] = f32x4{0.f, 0.f, 0.f, 0.f};
  wait_vm<16>();
  read_kf<0>(c, smem);
  phaseA<0, true>(c, smem, 0);
  phaseB<0, true>(c, smem, 0);
  for (int t = 1; t < NT - 1; t += 2) {
    phaseA<1, false>(c, smem, t);
    phaseB<1, false>(c, smem, t);
    phaseA<0, false>(c, smem, t + 1);
    phaseB<0, false>(c, smem, t + 1);
  }
  phaseA<1, false>(c, smem, NT - 1);
  phaseB<1, false>(c, smem, NT - 1);
  wait_vm<8>();
  read_vf<1>(c, smem);
#pragma unroll
  for (int k = 0; k < 27; ++k) sm2_step<1>(c, k);
  nop8();
#pragma unroll
  for (int qt = 0; qt < NQT; ++qt)
#pragma unroll
    for (int dt = 0; dt < NDT; ++dt) {
      const bf16x8 pf = __builtin_bit_cast(bf16x8, qt < 2 ? c.p01[1][qt] : c.p234[qt - 2]);
      mfma_pv(c.o[dt][qt], c.vf[dt], pf);
    }
  nop8();
  wait_vm<0>();
#pragma unroll
  for (int qt = 0; qt < NQT; ++qt) c.l[qt] = xl_sum(c.l[qt]);
  if (lane < 16) {
#pragma unroll
    for (int qt = 0; qt < NQT; ++qt) *reinterpret_cast<f32x2*>(smem + ML_OFF + (wave * SQ + qt * 16 + lane) * 8) = f32x2{c.mc[qt], c.l[qt]};
  }
  __syncthreads();
  float fsc[NQT];
#pragma unroll
  for (int qt = 0; qt < NQT; ++qt) {
    f32x2 v[4];
#pragma unroll
    for (int w = 0; w < 4; ++w) v[w] = *reinterpret_cast<const f32x2*>(smem + ML_OFF + (w * SQ + qt * 16 + l15) * 8);
    const float M = fmaxf(fmaxf(v[0][0], v[1][0]), fmaxf(v[2][0], v[3][0]));
    float L = 0.f;
#pragma unroll
    for (int w = 0; w < 4; ++w) L += v[w][1] * __builtin_amdgcn_exp2f(v[w][0] - M);
    const float f = __builtin_amdgcn_exp2f(c.mc[qt] - M) / L;
    fsc[qt] = f;
  }
#pragma unroll
  for (int tw = 0; tw < 4; ++tw) {
    if (tw != wave) {
      const int sl = wave < tw ? wave : wave - 1;
#pragma unroll
      for (int dl = 0; dl < 2; ++dl)
#pragma unroll
        for (int qt = 0; qt < NQT; ++qt)
        { f32x4 t = c.o[2 * tw + dl][qt]; asm volatile("" : "+v"(t)); t *= fsc[qt]; asm volatile("" : "+v"(t));
          *reinterpret_cast<f32x4*>(smem + tw * XREG + sl * 10240 + ((dl * 5 + qt) * 64 + lane) * 16) = t; }
    }
  }
  __syncthreads();
  int l15e = l15, ge = g; asm volatile("" : "+v"(l15e), "+v"(ge));
  uint16_t* Oh = Og + (size_t)bq * SQ * DH;
#pragma unroll
  for (int tw = 0; tw < 4; ++tw) {
    if (tw == wave) {
#pragma unroll
      for (int dl = 0; dl < 2; ++dl)
#pragma unroll
        for (int qt = 0; qt < NQT; ++qt) {
          f32x4 a = c.o[2 * tw + dl][qt]; asm volatile("" : "+v"(a)); a *= fsc[qt];
#pragma unroll
          for (int sl = 0; sl < 3; ++sl) a += *reinterpret_cast<const f32x4*>(smem + tw * XREG + sl * 10240 + ((dl * 5 + qt) * 64 + lane) * 16);
          u32x2 w = {cvtpk(a[0], a[1]), cvtpk(a[2], a[3])};
          *reinterpret_cast<u32x2*>(Oh + (qt * 16 + l15e) * DH + 32 * tw + 16 * dl + 4 * ge) = w;
        }
    }
  }
}
}

extern "C" void kernel_launch(void* const* d_in, const int* in_sizes, int n_in, void* d_out, int out_size, void* d_ws, size_t ws_size,
                              hipStream_t stream) {
  (void)in_sizes; (void)n_in; (void)out_size; (void)d_ws; (void)ws_size;
  gqa::attn_fwd<<<dim3(gqa::NB * gqa::HQ), dim3(256), 0, stream>>>((const uint16_t*)d_in[0], (const uint16_t*)d_in[1], (const uint16_t*)d_in[2], (uint16_t*)d_out);
}
```
